```python
import torch

def _softmax(x, axis=-1):
    return torch.softmax(x, dim=axis)

def _gelu(x):
    return torch.nn.functional.gelu(x, approximate='tanh')

def _silu(x):
    return torch.nn.functional.silu(x)

def _sigmoid(x):
    return torch.sigmoid(x)

def _layernorm(x, axis=-1, eps=1e-05):
    m = x.mean(dim=axis, keepdim=True)
    v = x.var(dim=axis, keepdim=True, unbiased=False)
    return (x - m) / torch.sqrt(v + eps)

def _scatter_add(tgt, idx, src, axis=0):
    return tgt.clone().index_add_(axis, idx, src)

def _segment_sum(src, seg, n):
    out = torch.zeros((n,) + src.shape[1:], dtype=src.dtype)
    return out.index_add_(0, seg, src)

def _pad(x, pw):
    flat = tuple((v for pair in reversed(pw) for v in pair))
    return torch.nn.functional.pad(x, flat)
kernel_name = 'adaLN_cond__ssm_conv_s4__dense_activation_silu__prompt'

def setup_inputs():

    _w = lambda a: (lambda t: t.to(torch.bfloat16) if t.is_floating_point() else t)(torch.as_tensor(a))

    def _A_setup():
        B, L, D, N = (1, 2048, 576, 128)
        A_re = -(lambda _x: torch.exp(torch.as_tensor(_x)))((lambda *s: torch.randn(*s, dtype=torch.bfloat16) * 3.2)(D, N))
        A_im = torch.pi * (lambda *s: torch.randn(*s, dtype=torch.bfloat16) * 0.05)(D, N)
        B_re = (lambda *s: torch.randn(*s, dtype=torch.bfloat16) * 0.1)(D, N)
        B_im = (lambda *s: torch.randn(*s, dtype=torch.bfloat16) * 0.1)(D, N)
        C_re = (lambda *s: torch.randn(*s, dtype=torch.bfloat16) * 0.1)(D, N)
        C_im = (lambda *s: torch.randn(*s, dtype=torch.bfloat16) * 0.1)(D, N)
        log_dt = (lambda *s: torch.randn(*s, dtype=torch.bfloat16) * 0.05)(D) * 0.5 - 4.0
        Dskip = (lambda *s: torch.randn(*s, dtype=torch.bfloat16) * 0.05)(D)
        return {'u': _w((lambda *s: torch.randn(*s, dtype=torch.bfloat16) * 0.05)(B, L, D)), 'A_re': _w(A_re), 'A_im': _w(A_im), 'B_re': _w(B_re), 'B_im': _w(B_im), 'C_re': _w(C_re), 'C_im': _w(C_im), 'log_dt': _w(log_dt), 'Dskip': _w(Dskip)}
    _A_in = _A_setup()

    def _B_setup():
        B, S, D, F = (1, 2048, 576, 4608)
        return {'x': _w((lambda *s: torch.randn(*s, dtype=torch.bfloat16) * 0.05)(B, S, D)), 'w1': _w((lambda *s: torch.randn(*s, dtype=torch.bfloat16) * 3.2)(D, F) * D ** (-0.5)), 'w2': _w((lambda *s: torch.randn(*s, dtype=torch.bfloat16) * 0.4)(F, D) * F ** (-0.5))}
    _B_in = _B_setup()
    return {'A_u': _A_in['u'], 'A_A_re': _A_in['A_re'], 'A_A_im': _A_in['A_im'], 'A_B_re': _A_in['B_re'], 'A_B_im': _A_in['B_im'], 'A_C_re': _A_in['C_re'], 'A_C_im': _A_in['C_im'], 'A_log_dt': _A_in['log_dt'], 'A_Dskip': _A_in['Dskip'], 'B_x': _B_in['x'], 'B_w1': _B_in['w1'], 'B_w2': _B_in['w2'], 'c': _w((lambda *s: torch.randn(*s, dtype=torch.bfloat16) * 0.2)(1, 576) * 8.0), 'w_mod': _w((lambda *s: torch.randn(*s, dtype=torch.bfloat16) * 0.2)(576, 6 * 576) * 576 ** (-0.5) * 8.0)}

def reference(A_u, A_A_re, A_A_im, A_B_re, A_B_im, A_C_re, A_C_im, A_log_dt, A_Dskip, B_x, B_w1, B_w2, c, w_mod):

    def _A_ref(u, A_re, A_im, B_re, B_im, C_re, C_im, log_dt, Dskip):
        Bsz, L, D = u.shape
        dt = torch.exp(log_dt)[:, None]
        A = A_re + 1j * A_im
        Bc = B_re + 1j * B_im
        Cc = C_re + 1j * C_im
        logdA = dt * A
        dB = dt * Bc
        l = (lambda x, dt: x.to(dt))(torch.arange(L), A_re.dtype)
        P = torch.exp(logdA[:, :, None] * l[None, None, :])
        K = torch.einsum('dn,dnl,dn->dl', Cc, P, dB).real
        L2 = 2 * L
        Kp = _pad(K, ((0, 0), (0, L)))
        up = (lambda x, *d: x.permute(*d))(u, 0, 2, 1)
        up = _pad(up, ((0, 0), (0, 0), (0, L)))
        Kf = (lambda x, axes=(-2, -1): torch.fft.rfft2(x.float(), dim=axes))(Kp[:, None, :], axes=(-2, -1))
        Uf = (lambda x, axes=(-2, -1): torch.fft.rfft2(x.float(), dim=axes))(up[:, :, None, :], axes=(-2, -1))
        Yf = Uf * Kf[None, :, :, :]
        y = (lambda x, s, axes=(-2, -1): torch.fft.irfft2(x, s=s, dim=axes).to(torch.bfloat16))(Yf, (1, L2), axes=(-2, -1))[:, :, 0, :L]
        y = (lambda x, *d: x.permute(*d))(y, 0, 2, 1) + u * Dskip[None, None, :]
        return y

    def _B_ref(x, w1, w2):
        h = torch.einsum('bsd,df->bsf', x, w1)
        h = _silu(h)
        out = torch.einsum('bsf,fd->bsd', h, w2)
        return out
    x = A_u
    mod = (_silu(c) @ w_mod).reshape(c.shape[0], 6, 1, -1)
    sa, ca, ga, sb, cb, gb = (mod[:, 0].clone(), mod[:, 1].clone(), mod[:, 2].clone(), mod[:, 3].clone(), mod[:, 4].clone(), mod[:, 5].clone())
    h = _layernorm(x, axis=-1) * (1 + ca) + sa
    x = x + ga * _A_ref(h, A_A_re, A_A_im, A_B_re, A_B_im, A_C_re, A_C_im, A_log_dt, A_Dskip)
    h = _layernorm(x, axis=-1) * (1 + cb) + sb
    x = x + gb * _B_ref(h, B_w1, B_w2)
    return x
```

```cpp
#include <hip/hip_runtime.h>
#include <hip/hip_bf16.h>
#include <stdint.h>

constexpr int L_ = 2048;
constexpr int D_ = 576;
constexpr int NS_ = 128;
constexpr int F_ = 4608;
constexpr int NQ_ = 641;
constexpr int NQP_ = 644;

typedef unsigned short u16;
using bf16x8 = __attribute__((ext_vector_type(8))) short;
using f32x4 = __attribute__((ext_vector_type(4))) float;
using f32x16 = __attribute__((ext_vector_type(16))) float;

__device__ __forceinline__ float bf2f(u16 v) { return __uint_as_float(((unsigned)v) << 16); }
__device__ __forceinline__ u16 f2bf(float f) {
  __hip_bfloat16 b = __float2bfloat16(f);
  return *reinterpret_cast<u16*>(&b);
}
__device__ __forceinline__ float rbf(float f) { return bf2f(f2bf(f)); }

constexpr size_t WS_MOD = 0;
constexpr size_t WS_KQ = 16384;
constexpr size_t WS_XNT = WS_KQ + (size_t)D_ * NQP_ * 4;
constexpr size_t WS_ST = WS_XNT + (size_t)D_ * L_ * 2;
constexpr size_t WS_X2 = WS_ST + (size_t)D_ * L_ * 2;
constexpr size_t WS_H2 = WS_X2 + (size_t)D_ * L_ * 2;
constexpr size_t WS_W1T = WS_H2 + (size_t)D_ * L_ * 2;
constexpr size_t WS_W2T = WS_W1T + (size_t)D_ * F_ * 2;
constexpr size_t WS_HID = WS_W2T + (size_t)D_ * F_ * 2;
constexpr size_t WS_PART = WS_HID + (size_t)L_ * F_ * 2;
constexpr int SPLITK2 = 4;

constexpr int A_THREADS = 320;
constexpr int A_NK = D_;
constexpr int A_NM = 54;
constexpr int A_NL = 64;
constexpr int A_NT = 1296;
constexpr int A_GRID = A_NK + A_NM + A_NL + A_NT;

struct PrepArgs {
  const u16 *x, *Are, *Aim, *Bre, *Bim, *Cre, *Cim, *logdt, *c, *wmod, *w1, *w2;
  char* ws;
};

__global__ __launch_bounds__(A_THREADS) void prep_kernel(PrepArgs p) {
  __shared__ __attribute__((aligned(16))) float sm[10000];
  const int tid = threadIdx.x;
  int bid = blockIdx.x;
  if (bid < A_NK) {
    const int d = bid;
    float* s_a = sm;
    float* s_b = sm + 128;
    float* s_wr = sm + 256;
    float* s_wi = sm + 384;
    float* s_p = sm + 512;
    float* s_part = sm + 512 + 1024;
    if (tid < NS_) {
      float dt = rbf(__expf(bf2f(p.logdt[d])));
      dt = rbf(expf(bf2f(p.logdt[d])));
      int idx = d * NS_ + tid;
      float are = bf2f(p.Are[idx]), aim = bf2f(p.Aim[idx]);
      float bre = bf2f(p.Bre[idx]), bim = bf2f(p.Bim[idx]);
      float cre = bf2f(p.Cre[idx]), cim = bf2f(p.Cim[idx]);
      float a = dt * are, b = dt * aim;
      float dbr = dt * bre, dbi = dt * bim;
      s_a[tid] = a; s_b[tid] = b;
      s_wr[tid] = cre * dbr - cim * dbi;
      s_wi[tid] = cre * dbi + cim * dbr;
    }
    __syncthreads();
    for (int i = tid; i < 4 * NS_; i += A_THREADS) {
      int reg = i >> 7, n = i & 127;
      float st = (float)(1 << reg);
      float e = expf(s_a[n] * st);
      float sn, cs;
      sincosf(s_b[n] * st, &sn, &cs);
      s_p[(reg * 128 + n) * 2 + 0] = e * cs;
      s_p[(reg * 128 + n) * 2 + 1] = e * sn;
    }
    __syncthreads();
    const int ng = tid / 40, seg = tid % 40;
    int reg, l0;
    if (seg < 16) { reg = 0; l0 = seg * 16; }
    else if (seg < 24) { reg = 1; l0 = 256 + (seg - 16) * 32; }
    else if (seg < 32) { reg = 2; l0 = 512 + (seg - 24) * 64; }
    else { reg = 3; l0 = 1024 + (seg - 32) * 128; }
    float acc[17];
#pragma unroll
    for (int i = 0; i < 17; ++i) acc[i] = 0.f;
    const float l0f = (float)l0;
    for (int nn = 0; nn < 16; ++nn) {
      int n = ng * 16 + nn;
      float a = s_a[n], b = s_b[n];
      float e = expf(a * l0f);
      float sn, cs;
      sincosf(b * l0f, &sn, &cs);
      float er = e * cs, ei = e * sn;
      float wr = s_wr[n], wi = s_wi[n];
      float zr = wr * er - wi * ei;
      float zi = wr * ei + wi * er;
      float pr = s_p[(reg * 128 + n) * 2 + 0], pi = s_p[(reg * 128 + n) * 2 + 1];
#pragma unroll
      for (int i = 0; i < 17; ++i) {
        acc[i] += zr;
        float nzr = zr * pr - zi * pi;
        float nzi = zr * pi + zi * pr;
        zr = nzr; zi = nzi;
      }
    }
#pragma unroll
    for (int i = 0; i < 16; ++i) s_part[ng * 648 + seg * 16 + i] = acc[i];
    if (seg == 39) s_part[ng * 648 + 640] = acc[16];
    __syncthreads();
    float* kq = (float*)(p.ws + WS_KQ) + (size_t)d * NQP_;
    for (int i = tid; i < NQ_; i += A_THREADS) {
      float s = 0.f;
#pragma unroll
      for (int g = 0; g < 8; ++g) s += s_part[g * 648 + i];
      kq[i] = s;
    }
    return;
  }
  bid -= A_NK;
  if (bid < A_NM) {
    float* red = sm;
    const int col0 = bid * 64;
    if (tid < 256) {
      int kg = tid >> 3, cg = tid & 7;
      float acc[8];
#pragma unroll
      for (int j = 0; j < 8; ++j) acc[j] = 0.f;
      for (int k = kg; k < D_; k += 32) {
        float cv = bf2f(p.c[k]);
        float sc = rbf(cv / (1.f + expf(-cv)));
        uint4 w = *reinterpret_cast<const uint4*>(p.wmod + (size_t)k * 3456 + col0 + cg * 8);
        unsigned ww[4] = {w.x, w.y, w.z, w.w};
#pragma unroll
        for (int j = 0; j < 4; ++j) {
          acc[2 * j] += sc * __uint_as_float(ww[j] << 16);
          acc[2 * j + 1] += sc * __uint_as_float(ww[j] & 0xffff0000u);
        }
      }
#pragma unroll
      for (int j = 0; j < 8; ++j) red[kg * 64 + cg * 8 + j] = acc[j];
    }
    __syncthreads();
    if (tid < 64) {
      float s = 0.f;
      for (int g = 0; g < 32; ++g) s += red[g * 64 + tid];
      ((float*)(p.ws + WS_MOD))[col0 + tid] = rbf(s);
    }
    return;
  }
  bid -= A_NM;
  if (bid < A_NL) {
    u16* tile = (u16*)sm;
    const int t0 = bid * 32;
    const int wave = tid >> 6, lane = tid & 63;
    for (int tt = wave; tt < 32; tt += 5) {
      const u16* xr = p.x + (size_t)(t0 + tt) * D_;
      float v[9];
      float s = 0.f;
#pragma unroll
      for (int j = 0; j < 9; ++j) { v[j] = bf2f(xr[j * 64 + lane]); s += v[j]; }
#pragma unroll
      for (int o = 32; o > 0; o >>= 1) s += __shfl_xor(s, o);
      float mean = s * (1.f / D_);
      float q = 0.f;
#pragma unroll
      for (int j = 0; j < 9; ++j) { float dlt = v[j] - mean; q += dlt * dlt; }
#pragma unroll
      for (int o = 32; o > 0; o >>= 1) q += __shfl_xor(q, o);
      float var = rbf(q * (1.f / D_));
      float mb = rbf(mean);
      float den = rbf(sqrtf(rbf(var + 1e-5f)));
#pragma unroll
      for (int j = 0; j < 9; ++j) {
        float xn = rbf(rbf(v[j] - mb) / den);
        tile[(j * 64 + lane) * 34 + tt] = f2bf(xn);
      }
    }
    __syncthreads();
    u16* xnT = (u16*)(p.ws + WS_XNT);
    for (int i = tid; i < D_ * 16; i += A_THREADS) {
      int dd = i >> 4, w = i & 15;
      unsigned val = *reinterpret_cast<const unsigned*>(&tile[dd * 34 + w * 2]);
      *reinterpret_cast<unsigned*>(xnT + (size_t)dd * L_ + t0 + w * 2) = val;
    }
    return;
  }
  bid -= A_NL;
  {
    u16* tile = (u16*)sm;
    const u16* src; u16* dst; int R, C, tr, tc;
    if (bid < 648) {
      src = p.w1; dst = (u16*)(p.ws + WS_W1T); R = D_; C = F_;
      tr = bid / 72; tc = bid % 72;
    } else {
      bid -= 648;
      src = p.w2; dst = (u16*)(p.ws + WS_W2T); R = F_; C = D_;
      tr = bid / 9; tc = bid % 9;
    }
    for (int i = tid; i < 64 * 32; i += A_THREADS) {
      int r = i >> 5, cw = i & 31;
      unsigned val = *reinterpret_cast<const unsigned*>(src + (size_t)(tr * 64 + r) * C + tc * 64 + cw * 2);
      tile[r * 66 + cw * 2] = (u16)(val & 0xffff);
      tile[r * 66 + cw * 2 + 1] = (u16)(val >> 16);
    }
    __syncthreads();
    for (int i = tid; i < 64 * 32; i += A_THREADS) {
      int c = i >> 5, rw = i & 31;
      unsigned val = (unsigned)tile[(rw * 2) * 66 + c] | ((unsigned)tile[(rw * 2 + 1) * 66 + c] << 16);
      *reinterpret_cast<unsigned*>(dst + (size_t)(tc * 64 + c) * R + tr * 64 + rw * 2) = val;
    }
  }
}

constexpr int CV_THREADS = 192;
constexpr int CV_COPY_ELEMS = 2112;
constexpr int CV_COPY_STRIDE = CV_COPY_ELEMS * 2 + 32;
constexpr int CV_KCOL_STRIDE = 80;
constexpr int CV_LDS_COPIES = 8 * CV_COPY_STRIDE;
constexpr int CV_LDS_KHI = CV_LDS_COPIES;
constexpr int CV_LDS_KLO = CV_LDS_KHI + 64 * CV_KCOL_STRIDE;
constexpr int CV_LDS_ZERO = CV_LDS_KLO + 64 * CV_KCOL_STRIDE;
constexpr int CV_LDS_U = CV_LDS_ZERO + 64;
constexpr int CV_LDS_RED = CV_LDS_U + 4096;
constexpr int CV_LDS_TOTAL = CV_LDS_RED + 4096;

__device__ __forceinline__ int kq_index(int l) {
  if (l < 256) return l;
  int sh = (l < 512) ? 1 : (l < 1024) ? 2 : 3;
  int q = l >> sh;
  int rem = l & ((1 << sh) - 1);
  int half = 1 << (sh - 1);
  if (rem > half || (rem == half && (q & 1))) q += 1;
  int base = 256 + (sh - 1) * 128;
  return base + (q - 128);
}

__global__ __launch_bounds__(CV_THREADS) void conv_kernel(const char* __restrict__ ws_c, char* __restrict__ ws,
                                                          const u16* __restrict__ Dskip) {
  extern __shared__ __attribute__((aligned(16))) char lds[];
  const int d = blockIdx.x;
  const int tid = threadIdx.x;
  const float* mod = (const float*)(ws_c + WS_MOD);
  const u16* xn = (const u16*)(ws_c + WS_XNT) + (size_t)d * L_;
  const float* kq = (const float*)(ws_c + WS_KQ) + (size_t)d * NQP_;
  const float sa = mod[d], ca1 = rbf(1.f + mod[D_ + d]);
  const float dsk = bf2f(Dskip[d]);
  u16* su = (u16*)(lds + CV_LDS_U);
  for (int t = tid; t < L_; t += CV_THREADS) {
    float h = rbf(rbf(bf2f(xn[t]) * ca1) + sa);
    su[t] = f2bf(h);
  }
  if (tid < 16) ((unsigned*)(lds + CV_LDS_ZERO))[tid] = 0u;
  for (int l = tid; l < L_; l += CV_THREADS) {
    float kv = kq[kq_index(l)];
    u16 hi = f2bf(kv);
    u16 lo = f2bf(kv - bf2f(hi));
    int c = l >> 5, e = l & 31;
    *(u16*)(lds + CV_LDS_KHI + c * CV_KCOL_STRIDE + e * 2) = hi;
    *(u16*)(lds + CV_LDS_KLO + c * CV_KCOL_STRIDE + e * 2) = lo;
  }
  __syncthreads();
  for (int i = tid; i < 8 * CV_COPY_ELEMS; i += CV_THREADS) {
    int s = i / CV_COPY_ELEMS, pos = i % CV_COPY_ELEMS;
    int ui = 2047 - (pos + s);
    u16 v = (ui >= 0) ? su[ui] : (u16)0;
    *(u16*)(lds + s * CV_COPY_STRIDE + pos * 2) = v;
  }
  __syncthreads();
  const int wave = tid >> 6, lane = tid & 63;
  const int r = lane & 31, h = lane >> 5;
  const int tile = (wave == 0) ? 0 : 1;
  const int dlo = (wave == 2) ? 32 : 0;
  f32x16 acc = {};
  const int a = tile * 32 + r;
  for (int dd = 0; dd < 32; ++dd) {
    const int delta = dlo + dd;
#pragma unroll
    for (int ks = 0; ks < 2; ++ks) {
      int i0 = 2047 - (32 * delta + r - 16 * ks - 8 * h);
      int s = i0 & 7;
      const bf16x8 af = *reinterpret_cast<const bf16x8*>(lds + s * CV_COPY_STRIDE + (i0 - s) * 2);
      int c = a - delta;
      int offh = (c >= 0) ? (CV_LDS_KHI + c * CV_KCOL_STRIDE + (16 * ks + 8 * h) * 2) : CV_LDS_ZERO;
      int offl = (c >= 0) ? (CV_LDS_KLO + c * CV_KCOL_STRIDE + (16 * ks + 8 * h) * 2) : CV_LDS_ZERO;
      const bf16x8 bh = *reinterpret_cast<const bf16x8*>(lds + offh);
      const bf16x8 bl = *reinterpret_cast<const bf16x8*>(lds + offl);
      acc = __builtin_amdgcn_mfma_f32_32x32x16_bf16(af, bh, acc, 0, 0, 0);
      acc = __builtin_amdgcn_mfma_f32_32x32x16_bf16(af, bl, acc, 0, 0, 0);
    }
  }
  float* red = (float*)(lds + CV_LDS_RED);
  if (wave == 2) {
#pragma unroll
    for (int i = 0; i < 16; ++i) red[i * 64 + lane] = acc[i];
  }
  __syncthreads();
  if (wave < 2) {
    if (wave == 1) {
#pragma unroll
      for (int i = 0; i < 16; ++i) acc[i] += red[i * 64 + lane];
    }
    u16* sT = (u16*)(ws + WS_ST) + (size_t)d * L_;
#pragma unroll
    for (int g = 0; g < 4; ++g) {
      int b0 = 8 * g + 4 * h;
      int t = 32 * a + b0;
      u16 o[4];
#pragma unroll
      for (int j = 0; j < 4; ++j) {
        float y = rbf(acc[g * 4 + j]);
        float hv = bf2f(su[t + j]);
        o[j] = f2bf(y + rbf(hv * dsk));
      }
      uint2 pk;
      pk.x = (unsigned)o[0] | ((unsigned)o[1] << 16);
      pk.y = (unsigned)o[2] | ((unsigned)o[3] << 16);
      *reinterpret_cast<uint2*>(sT + t) = pk;
    }
  }
}

constexpr int LN2_TOK = 16;
__global__ __launch_bounds__(256) void ln2_kernel(const u16* __restrict__ x, char* __restrict__ ws) {
  __shared__ u16 tile[D_ * (LN2_TOK + 2)];
  const int t0 = blockIdx.x * LN2_TOK;
  const int tid = threadIdx.x;
  const u16* sT = (const u16*)(ws + WS_ST);
  const float* mod = (const float*)(ws + WS_MOD);
  for (int i = tid; i < D_ * (LN2_TOK / 2); i += 256) {
    int dd = i / (LN2_TOK / 2), w = i % (LN2_TOK / 2);
    unsigned val = *reinterpret_cast<const unsigned*>(sT + (size_t)dd * L_ + t0 + w * 2);
    *reinterpret_cast<unsigned*>(&tile[dd * (LN2_TOK + 2) + w * 2]) = val;
  }
  __syncthreads();
  const int wave = tid >> 6, lane = tid & 63;
  u16* x2 = (u16*)(ws + WS_X2);
  u16* h2 = (u16*)(ws + WS_H2);
  for (int tt = wave; tt < LN2_TOK; tt += 4) {
    const int t = t0 + tt;
    float v[9];
    float s = 0.f;
#pragma unroll
    for (int j = 0; j < 9; ++j) {
      int dd = j * 64 + lane;
      float ga = mod[2 * D_ + dd];
      float sv = bf2f(tile[dd * (LN2_TOK + 2) + tt]);
      v[j] = rbf(bf2f(x[(size_t)t * D_ + dd]) + rbf(ga * sv));
      x2[(size_t)t * D_ + dd] = f2bf(v[j]);
      s += v[j];
    }
#pragma unroll
    for (int o = 32; o > 0; o >>= 1) s += __shfl_xor(s, o);
    float mean = s * (1.f / D_);
    float q = 0.f;
#pragma unroll
    for (int j = 0; j < 9; ++j) { float dl = v[j] - mean; q += dl * dl; }
#pragma unroll
    for (int o = 32; o > 0; o >>= 1) q += __shfl_xor(q, o);
    float var = rbf(q * (1.f / D_));
    float mb = rbf(mean);
    float den = rbf(sqrtf(rbf(var + 1e-5f)));
#pragma unroll
    for (int j = 0; j < 9; ++j) {
      int dd = j * 64 + lane;
      float xn = rbf(rbf(v[j] - mb) / den);
      float cb1 = rbf(1.f + mod[4 * D_ + dd]);
      float hv = rbf(rbf(xn * cb1) + mod[3 * D_ + dd]);
      h2[(size_t)t * D_ + dd] = f2bf(hv);
    }
  }
}

template <int BM, int BN, int EPI>
__global__ __launch_bounds__(BM * 2) void gemm_nt(const u16* __restrict__ A, const u16* __restrict__ Bt,
                                                  void* __restrict__ Cout, int N, int K, int kchunk) {
  constexpr int NT = BM * 2;
  constexpr int NFN = BN / 16;
  constexpr int A_BYTES = BM * 128, B_BYTES = BN * 128, BUF = A_BYTES + B_BYTES;
  extern __shared__ __attribute__((aligned(16))) char lds[];
  const int tid = threadIdx.x, wave = tid >> 6, lane = tid & 63;
  const int fr = lane & 15, fq = lane >> 4;
  const int bm = blockIdx.y * BM, bn = blockIdx.x * BN;
  const int k0 = blockIdx.z * kchunk;
  const int nt = kchunk / 64;
  f32x4 acc[2][NFN];
#pragma unroll
  for (int i = 0; i < 2; ++i)
#pragma unroll
    for (int j = 0; j < NFN; ++j) acc[i][j] = (f32x4){0.f, 0.f, 0.f, 0.f};

  auto stage = [&](int buf, int kt) {
    char* base = lds + buf * BUF;
    const int kk = k0 + kt * 64;
#pragma unroll
    for (int i = 0; i < (BM * 8) / NT; ++i) {
      int p = i * NT + tid;
      int row = p >> 3, c = (p & 7) ^ (row & 7);
      const u16* g = A + (size_t)(bm + row) * K + kk + c * 8;
      __builtin_amdgcn_global_load_lds((const unsigned*)g, (unsigned*)(base + p * 16), 16, 0, 0);
    }
#pragma unroll
    for (int i = 0; i < (BN * 8 + NT - 1) / NT; ++i) {
      int p = i * NT + tid;
      if (i * NT + (wave << 6) < BN * 8) {
        int row = p >> 3, c = (p & 7) ^ (row & 7);
        const u16* g = Bt + (size_t)(bn + row) * K + kk + c * 8;
        __builtin_amdgcn_global_load_lds((const unsigned*)g, (unsigned*)(base + A_BYTES + p * 16), 16, 0, 0);
      }
    }
  };

  stage(0, 0);
  asm volatile("s_waitcnt vmcnt(0)" ::: "memory");
  __syncthreads();
  int cur = 0;
  for (int t = 0; t < nt; ++t) {
    if (t + 1 < nt) stage(cur ^ 1, t + 1);
    const char* base = lds + cur * BUF;
#pragma unroll
    for (int kk = 0; kk < 2; ++kk) {
      bf16x8 af[2], bfr[NFN];
#pragma unroll
      for (int i = 0; i < 2; ++i) {
        int row = wave * 32 + i * 16 + fr;
        int c = (kk * 4 + fq) ^ (row & 7);
        af[i] = *reinterpret_cast<const bf16x8*>(base + row * 128 + c * 16);
      }
#pragma unroll
      for (int j = 0; j < NFN; ++j) {
        int row = j * 16 + fr;
        int c = (kk * 4 + fq) ^ (row & 7);
        bfr[j] = *reinterpret_cast<const bf16x8*>(base + A_BYTES + row * 128 + c * 16);
      }
#pragma unroll
      for (int i = 0; i < 2; ++i)
#pragma unroll
        for (int j = 0; j < NFN; ++j)
          acc[i][j] = __builtin_amdgcn_mfma_f32_16x16x32_bf16(af[i], bfr[j], acc[i][j], 0, 0, 0);
    }
    asm volatile("s_waitcnt vmcnt(0)" ::: "memory");
    __syncthreads();
    cur ^= 1;
  }
  if (EPI == 0) {
    u16* C = (u16*)Cout;
#pragma unroll
    for (int i = 0; i < 2; ++i)
#pragma unroll
      for (int j = 0; j < NFN; ++j)
#pragma unroll
        for (int e = 0; e < 4; ++e) {
          int row = bm + wave * 32 + i * 16 + fq * 4 + e;
          int col = bn + j * 16 + fr;
          float hv = rbf(acc[i][j][e]);
          float sv = hv / (1.f + __expf(-hv));
          C[(size_t)row * N + col] = f2bf(sv);
        }
  } else {
    float* C = (float*)Cout + (size_t)blockIdx.z * L_ * D_;
#pragma unroll
    for (int i = 0; i < 2; ++i)
#pragma unroll
      for (int j = 0; j < NFN; ++j)
#pragma unroll
        for (int e = 0; e < 4; ++e) {
          int row = bm + wave * 32 + i * 16 + fq * 4 + e;
          int col = bn + j * 16 + fr;
          C[(size_t)row * N + col] = acc[i][j][e];
        }
  }
}

__global__ __launch_bounds__(256) void final_kernel(const char* __restrict__ ws, u16* __restrict__ out) {
  const int i = (blockIdx.x * 256 + threadIdx.x) * 4;
  if (i >= L_ * D_) return;
  const float* part = (const float*)(ws + WS_PART);
  const float* mod = (const float*)(ws + WS_MOD);
  const u16* x2 = (const u16*)(ws + WS_X2);
  float4 s = *reinterpret_cast<const float4*>(part + i);
#pragma unroll
  for (int z = 1; z < SPLITK2; ++z) {
    float4 q = *reinterpret_cast<const float4*>(part + (size_t)z * L_ * D_ + i);
    s.x += q.x; s.y += q.y; s.z += q.z; s.w += q.w;
  }
  uint2 xv = *reinterpret_cast<const uint2*>(x2 + i);
  int dd = i % D_;
  float sv[4] = {s.x, s.y, s.z, s.w};
  u16 xx[4] = {(u16)(xv.x & 0xffff), (u16)(xv.x >> 16), (u16)(xv.y & 0xffff), (u16)(xv.y >> 16)};
  u16 o[4];
#pragma unroll
  for (int j = 0; j < 4; ++j) {
    float gb = mod[5 * D_ + dd + j];
    o[j] = f2bf(bf2f(xx[j]) + rbf(gb * rbf(sv[j])));
  }
  uint2 pk;
  pk.x = (unsigned)o[0] | ((unsigned)o[1] << 16);
  pk.y = (unsigned)o[2] | ((unsigned)o[3] << 16);
  *reinterpret_cast<uint2*>(out + i) = pk;
}

extern "C" void kernel_launch(void* const* d_in, const int* in_sizes, int n_in,
                              void* d_out, int out_size, void* d_ws, size_t ws_size,
                              hipStream_t stream) {
  PrepArgs pa;
  pa.x = (const u16*)d_in[0];
  pa.Are = (const u16*)d_in[1];
  pa.Aim = (const u16*)d_in[2];
  pa.Bre = (const u16*)d_in[3];
  pa.Bim = (const u16*)d_in[4];
  pa.Cre = (const u16*)d_in[5];
  pa.Cim = (const u16*)d_in[6];
  pa.logdt = (const u16*)d_in[7];
  const u16* Dskip = (const u16*)d_in[8];
  pa.w1 = (const u16*)d_in[10];
  pa.w2 = (const u16*)d_in[11];
  pa.c = (const u16*)d_in[12];
  pa.wmod = (const u16*)d_in[13];
  pa.ws = (char*)d_ws;
  char* ws = (char*)d_ws;

  prep_kernel<<<A_GRID, A_THREADS, 0, stream>>>(pa);
  hipFuncSetAttribute((const void*)conv_kernel, hipFuncAttributeMaxDynamicSharedMemorySize, CV_LDS_TOTAL);
  conv_kernel<<<D_, CV_THREADS, CV_LDS_TOTAL, stream>>>(ws, ws, Dskip);
  ln2_kernel<<<L_ / LN2_TOK, 256, 0, stream>>>(pa.x, ws);
  {
    constexpr int BM = 256, BN = 144;
    constexpr int SH = 2 * (BM + BN) * 128;
    hipFuncSetAttribute((const void*)gemm_nt<BM, BN, 0>, hipFuncAttributeMaxDynamicSharedMemorySize, SH);
    gemm_nt<BM, BN, 0><<<dim3(F_ / BN, L_ / BM, 1), BM * 2, SH, stream>>>(
        (const u16*)(ws + WS_H2), (const u16*)(ws + WS_W1T), ws + WS_HID, F_, D_, D_);
  }
  {
    constexpr int BM = 128, BN = 144;
    constexpr int SH = 2 * (BM + BN) * 128;
    hipFuncSetAttribute((const void*)gemm_nt<BM, BN, 1>, hipFuncAttributeMaxDynamicSharedMemorySize, SH);
    gemm_nt<BM, BN, 1><<<dim3(D_ / BN, L_ / BM, SPLITK2), BM * 2, SH, stream>>>(
        (const u16*)(ws + WS_HID), (const u16*)(ws + WS_W2T), ws + WS_PART, D_, F_, F_ / SPLITK2);
  }
  final_kernel<<<(L_ * D_ / 4 + 255) / 256, 256, 0, stream>>>(ws, (u16*)d_out);
}
```

```cpp
#include <hip/hip_runtime.h>
#include <hip/hip_bf16.h>
#include <stdint.h>

constexpr int L_ = 2048;
constexpr int D_ = 576;
constexpr int NS_ = 128;
constexpr int F_ = 4608;
constexpr int NQ_ = 641;
constexpr int NQP_ = 644;

typedef unsigned short u16;
using bf16x8 = __attribute__((ext_vector_type(8))) short;
using f32x4 = __attribute__((ext_vector_type(4))) float;
using f32x16 = __attribute__((ext_vector_type(16))) float;

__device__ __forceinline__ float bf2f(u16 v) { return __uint_as_float(((unsigned)v) << 16); }
__device__ __forceinline__ u16 f2bf(float f) {
  __hip_bfloat16 b = __float2bfloat16(f);
  return *reinterpret_cast<u16*>(&b);
}
__device__ __forceinline__ float rbf(float f) { return bf2f(f2bf(f)); }

constexpr size_t WS_MOD = 0;
constexpr size_t WS_KQ = 16384;
constexpr size_t WS_XNT = WS_KQ + (size_t)D_ * NQP_ * 4;
constexpr size_t WS_ST = WS_XNT + (size_t)D_ * L_ * 2;
constexpr size_t WS_X2 = WS_ST + (size_t)D_ * L_ * 2;
constexpr size_t WS_H2 = WS_X2 + (size_t)D_ * L_ * 2;
constexpr size_t WS_W1T = WS_H2 + (size_t)D_ * L_ * 2;
constexpr size_t WS_W2T = WS_W1T + (size_t)D_ * F_ * 2;
constexpr size_t WS_HID = WS_W2T + (size_t)D_ * F_ * 2;
constexpr size_t WS_PART = WS_HID + (size_t)L_ * F_ * 2;
constexpr int SPLITK2 = 4;

constexpr int A_THREADS = 320;
constexpr int A_NK = D_;
constexpr int A_NM = 54;
constexpr int A_NL = 64;
constexpr int A_NT = 1296;
constexpr int A_GRID = A_NK + A_NM + A_NL + A_NT;

struct PrepArgs {
  const u16 *x, *Are, *Aim, *Bre, *Bim, *Cre, *Cim, *logdt, *c, *wmod, *w1, *w2;
  char* ws;
};

__global__ __launch_bounds__(A_THREADS) void prep_kernel(PrepArgs p) {
  __shared__ __attribute__((aligned(16))) float sm[10000];
  const int tid = threadIdx.x;
  int bid = blockIdx.x;
  if (bid < A_NK) {
    const int d = bid;
    float* s_a = sm;
    float* s_b = sm + 128;
    float* s_wr = sm + 256;
    float* s_wi = sm + 384;
    float* s_p = sm + 512;
    float* s_part = sm + 512 + 1024;
    if (tid < NS_) {
      float dt = rbf(expf(bf2f(p.logdt[d])));
      int idx = d * NS_ + tid;
      float are = bf2f(p.Are[idx]), aim = bf2f(p.Aim[idx]);
      float bre = bf2f(p.Bre[idx]), bim = bf2f(p.Bim[idx]);
      float cre = bf2f(p.Cre[idx]), cim = bf2f(p.Cim[idx]);
      float a = dt * are, b = dt * aim;
      float dbr = dt * bre, dbi = dt * bim;
      s_a[tid] = a; s_b[tid] = b;
      s_wr[tid] = cre * dbr - cim * dbi;
      s_wi[tid] = cre * dbi + cim * dbr;
    }
    __syncthreads();
    for (int i = tid; i < 4 * NS_; i += A_THREADS) {
      int reg = i >> 7, n = i & 127;
      float st = (float)(1 << reg);
      float e = __expf(s_a[n] * st);
      float sn = __sinf(s_b[n] * st), cs = __cosf(s_b[n] * st);
      s_p[(reg * 128 + n) * 2 + 0] = e * cs;
      s_p[(reg * 128 + n) * 2 + 1] = e * sn;
    }
    __syncthreads();
    const int ng = tid / 40, seg = tid % 40;
    int reg, l0;
    if (seg < 16) { reg = 0; l0 = seg * 16; }
    else if (seg < 24) { reg = 1; l0 = 256 + (seg - 16) * 32; }
    else if (seg < 32) { reg = 2; l0 = 512 + (seg - 24) * 64; }
    else { reg = 3; l0 = 1024 + (seg - 32) * 128; }
    float acc[17];
#pragma unroll
    for (int i = 0; i < 17; ++i) acc[i] = 0.f;
    const float l0f = (float)l0;
    for (int nn = 0; nn < 16; nn += 4) {
      typedef __attribute__((ext_vector_type(2))) float f2;
      f2 zr[2], zi[2], pr[2], pi[2];
#pragma unroll
      for (int u = 0; u < 4; ++u) {
        int n = ng * 16 + nn + u;
        float a = s_a[n], b = s_b[n];
        float e = __expf(a * l0f);
        float sn = __sinf(b * l0f), cs = __cosf(b * l0f);
        float er = e * cs, ei = e * sn;
        float wr = s_wr[n], wi = s_wi[n];
        zr[u >> 1][u & 1] = wr * er - wi * ei;
        zi[u >> 1][u & 1] = wr * ei + wi * er;
        pr[u >> 1][u & 1] = s_p[(reg * 128 + n) * 2 + 0];
        pi[u >> 1][u & 1] = s_p[(reg * 128 + n) * 2 + 1];
      }
#pragma unroll
      for (int i = 0; i < 17; ++i) {
        f2 sum = zr[0] + zr[1];
        acc[i] += sum[0] + sum[1];
#pragma unroll
        for (int u = 0; u < 2; ++u) {
          f2 nzr = zr[u] * pr[u] - zi[u] * pi[u];
          f2 nzi = zr[u] * pi[u] + zi[u] * pr[u];
          zr[u] = nzr; zi[u] = nzi;
        }
      }
    }
#pragma unroll
    for (int i = 0; i < 16; ++i) s_part[ng * 648 + seg * 16 + i] = acc[i];
    if (seg == 39) s_part[ng * 648 + 640] = acc[16];
    __syncthreads();
    float* kq = (float*)(p.ws + WS_KQ) + (size_t)d * NQP_;
    for (int i = tid; i < NQ_; i += A_THREADS) {
      float s = 0.f;
#pragma unroll
      for (int g = 0; g < 8; ++g) s += s_part[g * 648 + i];
      kq[i] = s;
    }
    return;
  }
  bid -= A_NK;
  if (bid < A_NM) {
    float* red = sm;
    float* scs = sm + 40 * 64;
    const int col0 = bid * 64;
    for (int k = tid; k < D_; k += A_THREADS) {
      float cv = bf2f(p.c[k]);
      scs[k] = rbf(cv / (1.f + __expf(-cv)));
    }
    __syncthreads();
    {
      int kg = tid >> 3, cg = tid & 7;
      uint4 w[15];
#pragma unroll
      for (int it = 0; it < 15; ++it) {
        int k = kg + it * 40;
        w[it] = (k < D_) ? *reinterpret_cast<const uint4*>(p.wmod + (size_t)k * 3456 + col0 + cg * 8) : make_uint4(0u, 0u, 0u, 0u);
      }
      float acc[8];
#pragma unroll
      for (int j = 0; j < 8; ++j) acc[j] = 0.f;
#pragma unroll
      for (int it = 0; it < 15; ++it) {
        int k = kg + it * 40;
        float sc = (k < D_) ? scs[k] : 0.f;
        unsigned ww[4] = {w[it].x, w[it].y, w[it].z, w[it].w};
#pragma unroll
        for (int j = 0; j < 4; ++j) {
          acc[2 * j] += sc * __uint_as_float(ww[j] << 16);
          acc[2 * j + 1] += sc * __uint_as_float(ww[j] & 0xffff0000u);
        }
      }
#pragma unroll
      for (int j = 0; j < 8; ++j) red[kg * 64 + cg * 8 + j] = acc[j];
    }
    __syncthreads();
    if (tid < 64) {
      float s = 0.f;
      for (int g = 0; g < 40; ++g) s += red[g * 64 + tid];
      ((float*)(p.ws + WS_MOD))[col0 + tid] = rbf(s);
    }
    return;
  }
  bid -= A_NM;
  if (bid < A_NL) {
    u16* tile = (u16*)sm;
    const int t0 = bid * 32;
    const int wave = tid >> 6, lane = tid & 63;
    float xv[7][9];
#pragma unroll
    for (int r = 0; r < 7; ++r) {
      int tt = wave + r * 5;
      if (tt < 32) {
#pragma unroll
        for (int j = 0; j < 9; ++j) xv[r][j] = bf2f(p.x[(size_t)(t0 + tt) * D_ + j * 64 + lane]);
      }
    }
#pragma unroll
    for (int r = 0; r < 7; ++r) {
      int tt = wave + r * 5;
      if (tt >= 32) break;
      float v[9];
      float s = 0.f;
#pragma unroll
      for (int j = 0; j < 9; ++j) { v[j] = xv[r][j]; s += v[j]; }
#pragma unroll
      for (int o = 32; o > 0; o >>= 1) s += __shfl_xor(s, o);
      float mean = s * (1.f / D_);
      float q = 0.f;
#pragma unroll
      for (int j = 0; j < 9; ++j) { float dlt = v[j] - mean; q += dlt * dlt; }
#pragma unroll
      for (int o = 32; o > 0; o >>= 1) q += __shfl_xor(q, o);
      float var = rbf(q * (1.f / D_));
      float mb = rbf(mean);
      float den = rbf(sqrtf(rbf(var + 1e-5f)));
#pragma unroll
      for (int j = 0; j < 9; ++j) {
        float xn = rbf(rbf(v[j] - mb) / den);
        tile[(j * 64 + lane) * 34 + tt] = f2bf(xn);
      }
    }
    __syncthreads();
    u16* xnT = (u16*)(p.ws + WS_XNT);
    for (int i = tid; i < D_ * 16; i += A_THREADS) {
      int dd = i >> 4, w = i & 15;
      unsigned val = *reinterpret_cast<const unsigned*>(&tile[dd * 34 + w * 2]);
      *reinterpret_cast<unsigned*>(xnT + (size_t)dd * L_ + t0 + w * 2) = val;
    }
    return;
  }
  bid -= A_NL;
  {
    u16* tile = (u16*)sm;
    const u16* src; u16* dst; int R, C, tr, tc;
    if (bid < 648) {
      src = p.w1; dst = (u16*)(p.ws + WS_W1T); R = D_; C = F_;
      tr = bid / 72; tc = bid % 72;
    } else {
      bid -= 648;
      src = p.w2; dst = (u16*)(p.ws + WS_W2T); R = F_; C = D_;
      tr = bid / 9; tc = bid % 9;
    }
    for (int i = tid; i < 64 * 32; i += A_THREADS) {
      int r = i >> 5, cw = i & 31;
      unsigned val = *reinterpret_cast<const unsigned*>(src + (size_t)(tr * 64 + r) * C + tc * 64 + cw * 2);
      tile[r * 66 + cw * 2] = (u16)(val & 0xffff);
      tile[r * 66 + cw * 2 + 1] = (u16)(val >> 16);
    }
    __syncthreads();
    for (int i = tid; i < 64 * 32; i += A_THREADS) {
      int c = i >> 5, rw = i & 31;
      unsigned val = (unsigned)tile[(rw * 2) * 66 + c] | ((unsigned)tile[(rw * 2 + 1) * 66 + c] << 16);
      *reinterpret_cast<unsigned*>(dst + (size_t)(tc * 64 + c) * R + tr * 64 + rw * 2) = val;
    }
  }
}

constexpr int CV_THREADS = 256;
constexpr int CV_COPY_ELEMS = 2112;
constexpr int CV_COPY_STRIDE = CV_COPY_ELEMS * 2 + 32;
constexpr int CV_KCOL_STRIDE = 80;
constexpr int CV_LDS_COPIES = 8 * CV_COPY_STRIDE;
constexpr int CV_LDS_KHI = CV_LDS_COPIES;
constexpr int CV_LDS_KLO = CV_LDS_KHI + 64 * CV_KCOL_STRIDE;
constexpr int CV_LDS_ZERO = CV_LDS_KLO + 64 * CV_KCOL_STRIDE;
constexpr int CV_LDS_U = CV_LDS_ZERO + 64;
constexpr int CV_LDS_RED = CV_LDS_U + 4096;
constexpr int CV_LDS_TOTAL = CV_LDS_RED + 4096;

__device__ __forceinline__ int kq_index(int l) {
  if (l < 256) return l;
  int sh = (l < 512) ? 1 : (l < 1024) ? 2 : 3;
  int q = l >> sh;
  int rem = l & ((1 << sh) - 1);
  int half = 1 << (sh - 1);
  if (rem > half || (rem == half && (q & 1))) q += 1;
  int base = 256 + (sh - 1) * 128;
  return base + (q - 128);
}

__global__ __launch_bounds__(CV_THREADS) void conv_kernel(const char* __restrict__ ws_c, char* __restrict__ ws,
                                                          const u16* __restrict__ Dskip) {
  extern __shared__ __attribute__((aligned(16))) char lds[];
  const int d = blockIdx.x;
  const int tid = threadIdx.x;
  const float* mod = (const float*)(ws_c + WS_MOD);
  const u16* xn = (const u16*)(ws_c + WS_XNT) + (size_t)d * L_;
  const float* kq = (const float*)(ws_c + WS_KQ) + (size_t)d * NQP_;
  const float sa = mod[d], ca1 = rbf(1.f + mod[D_ + d]);
  const float dsk = bf2f(Dskip[d]);
  u16* su = (u16*)(lds + CV_LDS_U);
  {
    uint4 xv = *reinterpret_cast<const uint4*>(xn + tid * 8);
    unsigned w[4] = {xv.x, xv.y, xv.z, xv.w}, o[4];
#pragma unroll
    for (int j = 0; j < 4; ++j) {
      float h0 = rbf(rbf(__uint_as_float(w[j] << 16) * ca1) + sa);
      float h1 = rbf(rbf(__uint_as_float(w[j] & 0xffff0000u) * ca1) + sa);
      o[j] = (unsigned)f2bf(h0) | ((unsigned)f2bf(h1) << 16);
    }
    *reinterpret_cast<uint4*>(su + tid * 8) = make_uint4(o[0], o[1], o[2], o[3]);
  }
  if (tid < 16) ((unsigned*)(lds + CV_LDS_ZERO))[tid] = 0u;
  {
    float kv[8];
#pragma unroll
    for (int j = 0; j < 8; ++j) kv[j] = kq[kq_index(tid * 8 + j)];
    unsigned hi[4], lo[4];
#pragma unroll
    for (int j = 0; j < 4; ++j) {
      u16 h0 = f2bf(kv[2 * j]), h1 = f2bf(kv[2 * j + 1]);
      u16 l0 = f2bf(kv[2 * j] - bf2f(h0)), l1 = f2bf(kv[2 * j + 1] - bf2f(h1));
      hi[j] = (unsigned)h0 | ((unsigned)h1 << 16);
      lo[j] = (unsigned)l0 | ((unsigned)l1 << 16);
    }
    int c = tid >> 2, e = (tid & 3) * 8;
    *reinterpret_cast<uint4*>(lds + CV_LDS_KHI + c * CV_KCOL_STRIDE + e * 2) = make_uint4(hi[0], hi[1], hi[2], hi[3]);
    *reinterpret_cast<uint4*>(lds + CV_LDS_KLO + c * CV_KCOL_STRIDE + e * 2) = make_uint4(lo[0], lo[1], lo[2], lo[3]);
  }
  __syncthreads();
  for (int q = tid; q < CV_COPY_ELEMS / 8; q += CV_THREADS) {
    unsigned rd[8];
#pragma unroll
    for (int cc = 0; cc < 2; ++cc) {
      int qq = q + cc;
      uint4 uv = (qq < 256) ? *reinterpret_cast<const uint4*>(su + 2040 - 8 * qq) : make_uint4(0u, 0u, 0u, 0u);
      unsigned ud[4] = {uv.x, uv.y, uv.z, uv.w};
#pragma unroll
      for (int k = 0; k < 4; ++k) rd[cc * 4 + k] = (ud[3 - k] >> 16) | (ud[3 - k] << 16);
    }
#pragma unroll
    for (int sft = 0; sft < 8; ++sft) {
      unsigned o[4];
      if ((sft & 1) == 0) {
#pragma unroll
        for (int m = 0; m < 4; ++m) o[m] = rd[sft / 2 + m];
      } else {
#pragma unroll
        for (int m = 0; m < 4; ++m) o[m] = (rd[(sft - 1) / 2 + m] >> 16) | (rd[(sft - 1) / 2 + m + 1] << 16);
      }
      *reinterpret_cast<uint4*>(lds + sft * CV_COPY_STRIDE + q * 16) = make_uint4(o[0], o[1], o[2], o[3]);
    }
  }
  __syncthreads();
  const int wave = tid >> 6, lane = tid & 63;
  const int r = lane & 31, h = lane >> 5;
  const int tile = (wave == 0) ? 0 : 1;
  const int dlo = (wave == 2) ? 32 : 0;
  f32x16 acc = {};
  const int a = tile * 32 + r;
  for (int dd = 0; dd < ((wave < 3) ? 32 : 0); ++dd) {
    const int delta = dlo + dd;
#pragma unroll
    for (int ks = 0; ks < 2; ++ks) {
      int i0 = 2047 - (32 * delta + r - 16 * ks - 8 * h);
      int s = i0 & 7;
      const bf16x8 af = *reinterpret_cast<const bf16x8*>(lds + s * CV_COPY_STRIDE + (i0 - s) * 2);
      int c = a - delta;
      int offh = (c >= 0) ? (CV_LDS_KHI + c * CV_KCOL_STRIDE + (16 * ks + 8 * h) * 2) : CV_LDS_ZERO;
      int offl = (c >= 0) ? (CV_LDS_KLO + c * CV_KCOL_STRIDE + (16 * ks + 8 * h) * 2) : CV_LDS_ZERO;
      const bf16x8 bh = *reinterpret_cast<const bf16x8*>(lds + offh);
      const bf16x8 bl = *reinterpret_cast<const bf16x8*>(lds + offl);
      acc = __builtin_amdgcn_mfma_f32_32x32x16_bf16(af, bh, acc, 0, 0, 0);
      acc = __builtin_amdgcn_mfma_f32_32x32x16_bf16(af, bl, acc, 0, 0, 0);
    }
  }
  float* red = (float*)(lds + CV_LDS_RED);
  if (wave == 2) {
#pragma unroll
    for (int i = 0; i < 16; ++i) red[i * 64 + lane] = acc[i];
  }
  __syncthreads();
  if (wave < 2) {
    if (wave == 1) {
#pragma unroll
      for (int i = 0; i < 16; ++i) acc[i] += red[i * 64 + lane];
    }
    u16* sT = (u16*)(ws + WS_ST) + (size_t)d * L_;
#pragma unroll
    for (int g = 0; g < 4; ++g) {
      int b0 = 8 * g + 4 * h;
      int t = 32 * a + b0;
      u16 o[4];
#pragma unroll
      for (int j = 0; j < 4; ++j) {
        float y = rbf(acc[g * 4 + j]);
        float hv = bf2f(su[t + j]);
        o[j] = f2bf(y + rbf(hv * dsk));
      }
      uint2 pk;
      pk.x = (unsigned)o[0] | ((unsigned)o[1] << 16);
      pk.y = (unsigned)o[2] | ((unsigned)o[3] << 16);
      *reinterpret_cast<uint2*>(sT + t) = pk;
    }
  }
}

constexpr int LN2_TOK = 16;
__global__ __launch_bounds__(256) void ln2_kernel(const u16* __restrict__ x, char* __restrict__ ws) {
  __shared__ u16 tile[D_ * (LN2_TOK + 2)];
  const int t0 = blockIdx.x * LN2_TOK;
  const int tid = threadIdx.x;
  const u16* sT = (const u16*)(ws + WS_ST);
  const float* mod = (const float*)(ws + WS_MOD);
  const int wave = tid >> 6, lane = tid & 63;
  float xv[4][9], ga[9], cb1[9], sb[9];
#pragma unroll
  for (int tt = 0; tt < 4; ++tt)
#pragma unroll
    for (int j = 0; j < 9; ++j) xv[tt][j] = bf2f(x[(size_t)(t0 + wave * 4 + tt) * D_ + j * 64 + lane]);
#pragma unroll
  for (int j = 0; j < 9; ++j) {
    ga[j] = mod[2 * D_ + j * 64 + lane];
    sb[j] = mod[3 * D_ + j * 64 + lane];
    cb1[j] = mod[4 * D_ + j * 64 + lane];
  }
  for (int i = tid; i < D_ * (LN2_TOK / 2); i += 256) {
    int dd = i / (LN2_TOK / 2), w = i % (LN2_TOK / 2);
    unsigned val = *reinterpret_cast<const unsigned*>(sT + (size_t)dd * L_ + t0 + w * 2);
    *reinterpret_cast<unsigned*>(&tile[dd * (LN2_TOK + 2) + w * 2]) = val;
  }
  __syncthreads();
  u16* x2 = (u16*)(ws + WS_X2);
  u16* h2 = (u16*)(ws + WS_H2);
#pragma unroll
  for (int tt = 0; tt < 4; ++tt) {
    const int tl = wave * 4 + tt;
    const int t = t0 + tl;
    float v[9];
    float s = 0.f;
#pragma unroll
    for (int j = 0; j < 9; ++j) {
      int dd = j * 64 + lane;
      float sv = bf2f(tile[dd * (LN2_TOK + 2) + tl]);
      v[j] = rbf(xv[tt][j] + rbf(ga[j] * sv));
      x2[(size_t)t * D_ + dd] = f2bf(v[j]);
      s += v[j];
    }
#pragma unroll
    for (int o = 32; o > 0; o >>= 1) s += __shfl_xor(s, o);
    float mean = s * (1.f / D_);
    float q = 0.f;
#pragma unroll
    for (int j = 0; j < 9; ++j) { float dl = v[j] - mean; q += dl * dl; }
#pragma unroll
    for (int o = 32; o > 0; o >>= 1) q += __shfl_xor(q, o);
    float var = rbf(q * (1.f / D_));
    float mb = rbf(mean);
    float den = rbf(sqrtf(rbf(var + 1e-5f)));
#pragma unroll
    for (int j = 0; j < 9; ++j) {
      int dd = j * 64 + lane;
      float xn = rbf(rbf(v[j] - mb) / den);
      float hv = rbf(rbf(xn * rbf(1.f + cb1[j])) + sb[j]);
      h2[(size_t)t * D_ + dd] = f2bf(hv);
    }
  }
}

template <int BM, int BN, int EPI>
__global__ __launch_bounds__(BM * 2) void gemm_nt(const u16* __restrict__ A, const u16* __restrict__ Bt,
                                                  void* __restrict__ Cout, int N, int K, int kchunk) {
  constexpr int NT = BM * 2;
  constexpr int NFN = BN / 16;
  constexpr int A_BYTES = BM * 128, B_BYTES = BN * 128, BUF = A_BYTES + B_BYTES;
  constexpr int GA = (BM * 8) / NT;
  constexpr int GB_HI = (BN * 8 + NT - 1) / NT;
  constexpr int GB_LO = (BN * 8) / NT;
  constexpr int NWHI = ((BN * 8) % NT) / 64;
  extern __shared__ __attribute__((aligned(16))) char lds[];
  const int tid = threadIdx.x, wave = tid >> 6, lane = tid & 63;
  const int fr = lane & 15, fq = lane >> 4;
  const int bm = blockIdx.y * BM, bn = blockIdx.x * BN;
  const int k0 = blockIdx.z * kchunk;
  const int nt = kchunk / 64;
  const bool hiw = __builtin_amdgcn_readfirstlane(wave) < NWHI;
  f32x4 acc[2][NFN];
#pragma unroll
  for (int i = 0; i < 2; ++i)
#pragma unroll
    for (int j = 0; j < NFN; ++j) acc[i][j] = (f32x4){0.f, 0.f, 0.f, 0.f};

  const u16* ga[GA];
#pragma unroll
  for (int i = 0; i < GA; ++i) {
    int p = i * NT + tid;
    int row = p >> 3, c = (p & 7) ^ (row & 7);
    ga[i] = A + (size_t)(bm + row) * K + k0 + c * 8;
  }
  const u16* gb[GB_HI];
#pragma unroll
  for (int i = 0; i < GB_HI; ++i) {
    int p = i * NT + tid;
    int row = p >> 3, c = (p & 7) ^ (row & 7);
    if (row >= BN) row = BN - 1;
    gb[i] = Bt + (size_t)(bn + row) * K + k0 + c * 8;
  }
  auto stage = [&](int buf, int kt) {
    char* base = lds + buf * BUF;
#pragma unroll
    for (int i = 0; i < GA; ++i)
      __builtin_amdgcn_global_load_lds((const unsigned*)(ga[i] + kt * 64), (unsigned*)(base + (i * NT + tid) * 16), 16, 0, 0);
#pragma unroll
    for (int i = 0; i < GB_LO; ++i)
      __builtin_amdgcn_global_load_lds((const unsigned*)(gb[i] + kt * 64), (unsigned*)(base + A_BYTES + (i * NT + tid) * 16), 16, 0, 0);
    if (GB_HI > GB_LO && hiw)
      __builtin_amdgcn_global_load_lds((const unsigned*)(gb[GB_HI - 1] + kt * 64), (unsigned*)(base + A_BYTES + ((GB_HI - 1) * NT + tid) * 16), 16, 0, 0);
  };

  stage(0, 0);
  stage(1, 1);
  for (int t = 0; t < nt; ++t) {
    if (t + 1 < nt) {
      if (hiw) asm volatile("s_waitcnt vmcnt(%0)" ::"n"(GA + GB_HI) : "memory");
      else asm volatile("s_waitcnt vmcnt(%0)" ::"n"(GA + GB_LO) : "memory");
    } else {
      asm volatile("s_waitcnt vmcnt(0)" ::: "memory");
    }
    __builtin_amdgcn_s_barrier();
    if (t + 2 < nt) stage((t + 2) % 3, t + 2);
    const char* base = lds + (t % 3) * BUF;
#pragma unroll
    for (int kk = 0; kk < 2; ++kk) {
      bf16x8 af[2], bfr[NFN];
#pragma unroll
      for (int i = 0; i < 2; ++i) {
        int row = wave * 32 + i * 16 + fr;
        int c = (kk * 4 + fq) ^ (row & 7);
        af[i] = *reinterpret_cast<const bf16x8*>(base + row * 128 + c * 16);
      }
#pragma unroll
      for (int j = 0; j < NFN; ++j) {
        int row = j * 16 + fr;
        int c = (kk * 4 + fq) ^ (row & 7);
        bfr[j] = *reinterpret_cast<const bf16x8*>(base + A_BYTES + row * 128 + c * 16);
      }
      __builtin_amdgcn_s_setprio(1);
#pragma unroll
      for (int i = 0; i < 2; ++i)
#pragma unroll
        for (int j = 0; j < NFN; ++j)
          acc[i][j] = __builtin_amdgcn_mfma_f32_16x16x32_bf16(bfr[j], af[i], acc[i][j], 0, 0, 0);
      __builtin_amdgcn_s_setprio(0);
    }
    asm volatile("s_waitcnt lgkmcnt(0)" ::: "memory");
  }
  __builtin_amdgcn_s_barrier();
  if (EPI == 0) {
    constexpr int RS = BN * 2 + 16;
    char* tl = lds + wave * (32 * RS);
#pragma unroll
    for (int i = 0; i < 2; ++i)
#pragma unroll
      for (int j = 0; j < NFN; ++j) {
        u16 o[4];
#pragma unroll
        for (int e = 0; e < 4; ++e) {
          float hv = rbf(acc[i][j][e]);
          o[e] = f2bf(hv * __builtin_amdgcn_rcpf(1.f + __expf(-hv)));
        }
        uint2 pk;
        pk.x = (unsigned)o[0] | ((unsigned)o[1] << 16);
        pk.y = (unsigned)o[2] | ((unsigned)o[3] << 16);
        *reinterpret_cast<uint2*>(tl + (i * 16 + fr) * RS + (j * 16 + fq * 4) * 2) = pk;
      }
    asm volatile("s_waitcnt lgkmcnt(0)" ::: "memory");
    u16* C = (u16*)Cout;
    constexpr int CPR = BN / 8;
#pragma unroll
    for (int q = 0; q < (32 * CPR) / 64; ++q) {
      int ch = q * 64 + lane;
      int row = ch / CPR, c = ch % CPR;
      uint4 v = *reinterpret_cast<const uint4*>(tl + row * RS + c * 16);
      *reinterpret_cast<uint4*>(C + (size_t)(bm + wave * 32 + row) * N + bn + c * 8) = v;
    }
  } else {
    constexpr int RS = BN * 4 + 16;
    char* tl = lds + wave * (32 * RS);
#pragma unroll
    for (int i = 0; i < 2; ++i)
#pragma unroll
      for (int j = 0; j < NFN; ++j)
        *reinterpret_cast<f32x4*>(tl + (i * 16 + fr) * RS + (j * 16 + fq * 4) * 4) = acc[i][j];
    asm volatile("s_waitcnt lgkmcnt(0)" ::: "memory");
    float* C = (float*)Cout + (size_t)blockIdx.z * L_ * D_;
    constexpr int CPR = BN / 4;
#pragma unroll
    for (int q = 0; q < (32 * CPR) / 64; ++q) {
      int ch = q * 64 + lane;
      int row = ch / CPR, c = ch % CPR;
      uint4 v = *reinterpret_cast<const uint4*>(tl + row * RS + c * 16);
      *reinterpret_cast<uint4*>(C + (size_t)(bm + wave * 32 + row) * N + bn + c * 4) = v;
    }
  }
}

__global__ __launch_bounds__(256) void final_kernel(const char* __restrict__ ws, u16* __restrict__ out) {
  const int i = (blockIdx.x * 256 + threadIdx.x) * 4;
  if (i >= L_ * D_) return;
  const float* part = (const float*)(ws + WS_PART);
  const float* mod = (const float*)(ws + WS_MOD);
  const u16* x2 = (const u16*)(ws + WS_X2);
  float4 s = *reinterpret_cast<const float4*>(part + i);
#pragma unroll
  for (int z = 1; z < SPLITK2; ++z) {
    float4 q = *reinterpret_cast<const float4*>(part + (size_t)z * L_ * D_ + i);
    s.x += q.x; s.y += q.y; s.z += q.z; s.w += q.w;
  }
  uint2 xv = *reinterpret_cast<const uint2*>(x2 + i);
  int dd = i % D_;
  float sv[4] = {s.x, s.y, s.z, s.w};
  u16 xx[4] = {(u16)(xv.x & 0xffff), (u16)(xv.x >> 16), (u16)(xv.y & 0xffff), (u16)(xv.y >> 16)};
  u16 o[4];
#pragma unroll
  for (int j = 0; j < 4; ++j) {
    float gb = mod[5 * D_ + dd + j];
    o[j] = f2bf(bf2f(xx[j]) + rbf(gb * rbf(sv[j])));
  }
  uint2 pk;
  pk.x = (unsigned)o[0] | ((unsigned)o[1] << 16);
  pk.y = (unsigned)o[2] | ((unsigned)o[3] << 16);
  *reinterpret_cast<uint2*>(out + i) = pk;
}

extern "C" void kernel_launch(void* const* d_in, const int* in_sizes, int n_in,
                              void* d_out, int out_size, void* d_ws, size_t ws_size,
                              hipStream_t stream) {
  PrepArgs pa;
  pa.x = (const u16*)d_in[0];
  pa.Are = (const u16*)d_in[1];
  pa.Aim = (const u16*)d_in[2];
  pa.Bre = (const u16*)d_in[3];
  pa.Bim = (const u16*)d_in[4];
  pa.Cre = (const u16*)d_in[5];
  pa.Cim = (const u16*)d_in[6];
  pa.logdt = (const u16*)d_in[7];
  const u16* Dskip = (const u16*)d_in[8];
  pa.w1 = (const u16*)d_in[10];
  pa.w2 = (const u16*)d_in[11];
  pa.c = (const u16*)d_in[12];
  pa.wmod = (const u16*)d_in[13];
  pa.ws = (char*)d_ws;
  char* ws = (char*)d_ws;

  prep_kernel<<<A_GRID, A_THREADS, 0, stream>>>(pa);
  hipFuncSetAttribute((const void*)conv_kernel, hipFuncAttributeMaxDynamicSharedMemorySize, CV_LDS_TOTAL);
  conv_kernel<<<D_, CV_THREADS, CV_LDS_TOTAL, stream>>>(ws, ws, Dskip);
  ln2_kernel<<<L_ / LN2_TOK, 256, 0, stream>>>(pa.x, ws);
  {
    constexpr int BM = 256, BN = 144;
    constexpr int SH = 3 * (BM + BN) * 128;
    hipFuncSetAttribute((const void*)gemm_nt<BM, BN, 0>, hipFuncAttributeMaxDynamicSharedMemorySize, SH);
    gemm_nt<BM, BN, 0><<<dim3(F_ / BN, L_ / BM, 1), BM * 2, SH, stream>>>(
        (const u16*)(ws + WS_H2), (const u16*)(ws + WS_W1T), ws + WS_HID, F_, D_, D_);
  }
  {
    constexpr int BM = 128, BN = 144;
    constexpr int SH = 3 * (BM + BN) * 128;
    hipFuncSetAttribute((const void*)gemm_nt<BM, BN, 1>, hipFuncAttributeMaxDynamicSharedMemorySize, SH);
    gemm_nt<BM, BN, 1><<<dim3(D_ / BN, L_ / BM, SPLITK2), BM * 2, SH, stream>>>(
        (const u16*)(ws + WS_HID), (const u16*)(ws + WS_W2T), ws + WS_PART, D_, F_, F_ / SPLITK2);
  }
  final_kernel<<<(L_ * D_ / 4 + 255) / 256, 256, 0, stream>>>(ws, (u16*)d_out);
}
```
